```python
import torch

def _softmax(x, axis=-1):
    return torch.softmax(x, dim=axis)
def _gelu(x):
    return torch.nn.functional.gelu(x, approximate="tanh")
def _silu(x):
    return torch.nn.functional.silu(x)
def _sigmoid(x):
    return torch.sigmoid(x)
def _layernorm(x, axis=-1, eps=1e-5):
    m = x.mean(dim=axis, keepdim=True)
    v = x.var(dim=axis, keepdim=True, unbiased=False)
    return (x - m) / torch.sqrt(v + eps)
def _scatter_add(tgt, idx, src, axis=0):
    return tgt.clone().index_add_(axis, idx, src)
def _segment_sum(src, seg, n):
    out = torch.zeros((n,) + src.shape[1:], dtype=src.dtype)
    return out.index_add_(0, seg, src)
def _pad(x, pw):
    flat = tuple(v for pair in reversed(pw) for v in pair)
    return torch.nn.functional.pad(x, flat)

kernel_name = 'gated_mix__ssm_conv_s4__dense_activation_relu__prompt'

def setup_inputs():

    _w = (lambda a: (lambda t: t.to(torch.bfloat16) if t.is_floating_point() else t)(torch.as_tensor(a)))
    def _A_setup():
        B, L, D, N = 1, 2048, 512, 128
        A_re = -(lambda _x: torch.exp(torch.as_tensor(_x)))((lambda *s: torch.randn(*s, dtype=torch.bfloat16) * 3.2)(D, N))
        A_im = torch.pi * (lambda *s: torch.randn(*s, dtype=torch.bfloat16) * 0.2)(D, N)
        B_re = (lambda *s: torch.randn(*s, dtype=torch.bfloat16) * 0.4)(D, N)
        B_im = (lambda *s: torch.randn(*s, dtype=torch.bfloat16) * 0.4)(D, N)
        C_re = (lambda *s: torch.randn(*s, dtype=torch.bfloat16) * 0.4)(D, N)
        C_im = (lambda *s: torch.randn(*s, dtype=torch.bfloat16) * 0.4)(D, N)
        log_dt = (lambda *s: torch.randn(*s, dtype=torch.bfloat16) * 0.2)(D) * 0.5 - 4.0
        Dskip = (lambda *s: torch.randn(*s, dtype=torch.bfloat16) * 0.4)(D)
        return {
            "u": _w((lambda *s: torch.randn(*s, dtype=torch.bfloat16) * 0.05)(B, L, D)),
            "A_re": _w(A_re),
            "A_im": _w(A_im),
            "B_re": _w(B_re),
            "B_im": _w(B_im),
            "C_re": _w(C_re),
            "C_im": _w(C_im),
            "log_dt": _w(log_dt),
            "Dskip": _w(Dskip),
        }
    _A_in = _A_setup()

    def _B_setup():
        B, S, D, Dff = 1, 2048, 512, 1792
        return {
            "x": _w((lambda *s: torch.randn(*s, dtype=torch.bfloat16) * 0.05)(B, S, D)),
            "w1": _w((lambda *s: torch.randn(*s, dtype=torch.bfloat16) * 0.05)(D, Dff)),
            "w2": _w((lambda *s: torch.randn(*s, dtype=torch.bfloat16) * 0.05)(Dff, D)),
        }
    _B_in = _B_setup()

    return {
        "A_u": _A_in["u"],
        "A_A_re": _A_in["A_re"],
        "A_A_im": _A_in["A_im"],
        "A_B_re": _A_in["B_re"],
        "A_B_im": _A_in["B_im"],
        "A_C_re": _A_in["C_re"],
        "A_C_im": _A_in["C_im"],
        "A_log_dt": _A_in["log_dt"],
        "A_Dskip": _A_in["Dskip"],
        "B_x": _B_in["x"],
        "B_w1": _B_in["w1"],
        "B_w2": _B_in["w2"],
        "gate_w": _w((lambda *s: torch.randn(*s, dtype=torch.bfloat16) * 0.05)(512) * 16.0),
    }

def reference(A_u, A_A_re, A_A_im, A_B_re, A_B_im, A_C_re, A_C_im, A_log_dt, A_Dskip, B_x, B_w1, B_w2, gate_w):
    def _A_ref(u, A_re, A_im, B_re, B_im, C_re, C_im, log_dt, Dskip):
        Bsz, L, D = u.shape
        dt = torch.exp(log_dt)[:, None]
        A = A_re + 1j * A_im
        Bc = B_re + 1j * B_im
        Cc = C_re + 1j * C_im
        logdA = dt * A
        dB = dt * Bc
        l = (lambda x, dt: x.to(dt))(torch.arange(L), A_re.dtype)
        P = torch.exp(logdA[:, :, None] * l[None, None, :])
        K = torch.einsum("dn,dnl,dn->dl", Cc, P, dB).real
        L2 = 2 * L
        Kp = _pad(K, ((0, 0), (0, L)))
        up = (lambda x, *d: x.permute(*d))(u, 0, 2, 1)
        up = _pad(up, ((0, 0), (0, 0), (0, L)))
        Kf = (lambda x, axes=(-2,-1): torch.fft.rfft2(x.float(), dim=axes))(Kp[:, None, :], axes=(-2, -1))
        Uf = (lambda x, axes=(-2,-1): torch.fft.rfft2(x.float(), dim=axes))(up[:, :, None, :], axes=(-2, -1))
        Yf = Uf * Kf[None, :, :, :]
        y = (lambda x, s, axes=(-2,-1): torch.fft.irfft2(x, s=s, dim=axes).to(torch.bfloat16))(Yf, (1, L2), axes=(-2, -1))[:, :, 0, :L]
        y = (lambda x, *d: x.permute(*d))(y, 0, 2, 1) + u * Dskip[None, None, :]
        return y

    def _B_ref(x, w1, w2):
        h = torch.einsum("bsd,df->bsf", x, w1)
        zero = torch.zeros_like(h)
        h = torch.maximum(h, zero)
        return torch.einsum("bsf,fd->bsd", h, w2)

    x = A_u
    g = _sigmoid((x * gate_w).sum(-1, keepdims=True))
    a = _A_ref(x, A_A_re, A_A_im, A_B_re, A_B_im, A_C_re, A_C_im, A_log_dt, A_Dskip)
    b = _B_ref(x, B_w1, B_w2)
    return g * a + (1 - g) * b
```

```cpp
#include <hip/hip_runtime.h>
#include <hip/hip_bf16.h>
#include <stdint.h>

typedef unsigned short u16;
__device__ __forceinline__ float bf2f(u16 v){ return __uint_as_float(((unsigned)v)<<16); }
__device__ __forceinline__ u16 f2bf(float f){ unsigned u=__float_as_uint(f); u += 0x7FFF + ((u>>16)&1); return (u16)(u>>16); }
__device__ __forceinline__ float rbf(float f){ return bf2f(f2bf(f)); }

constexpr int L=2048, D=512, N=128, F=1792;

__global__ void k_gate(const u16* x, const u16* gw, float* g){
  int t=blockIdx.x; int lane=threadIdx.x;
  float s=0;
  for(int d=lane; d<D; d+=64) s += rbf(bf2f(x[t*D+d])*bf2f(gw[d]));
  for(int o=32;o>0;o>>=1) s += __shfl_xor(s,o);
  if(lane==0){ float z=rbf(s); g[t]=rbf(1.f/(1.f+expf(-z))); }
}
__global__ void k_kgen(const u16* Are,const u16* Aim,const u16* Bre,const u16* Bim,const u16* Cre,const u16* Cim,const u16* logdt,float* K){
  int d=blockIdx.x;
  __shared__ float lre[N], lim[N], cre[N], cim[N];
  float dt=rbf(expf(bf2f(logdt[d])));
  if(threadIdx.x<N){ int n=threadIdx.x;
    lre[n]=dt*bf2f(Are[d*N+n]); lim[n]=dt*bf2f(Aim[d*N+n]);
    float br=dt*bf2f(Bre[d*N+n]), bi=dt*bf2f(Bim[d*N+n]);
    float cr=bf2f(Cre[d*N+n]), ci=bf2f(Cim[d*N+n]);
    cre[n]=cr*br-ci*bi; cim[n]=cr*bi+ci*br;
  }
  __syncthreads();
  for(int l=threadIdx.x;l<L;l+=blockDim.x){
    float lf=rbf((float)l);
    float s=0;
    for(int n=0;n<N;n++){
      float e=expf(lre[n]*lf); float sn,cs; sincosf(lim[n]*lf,&sn,&cs);
      s += e*(cre[n]*cs - cim[n]*sn);
    }
    K[d*L+l]=s;
  }
}
__global__ void k_conv(const u16* u,const float* K,const u16* Dskip,float* a){
  int d=blockIdx.x;
  __shared__ float sk[L], su[L];
  for(int l=threadIdx.x;l<L;l+=blockDim.x){ sk[l]=K[d*L+l]; su[l]=bf2f(u[l*D+d]); }
  __syncthreads();
  float ds=bf2f(Dskip[d]);
  for(int t=threadIdx.x;t<L;t+=blockDim.x){
    float s=0; for(int j=0;j<=t;j++) s+=sk[j]*su[t-j];
    a[t*D+d]=rbf(rbf(s)+rbf(su[t]*ds));
  }
}
__global__ void k_gemm1(const u16* x,const u16* w1,u16* h){
  int t=blockIdx.x; __shared__ float sx[D];
  for(int i=threadIdx.x;i<D;i+=blockDim.x) sx[i]=bf2f(x[t*D+i]);
  __syncthreads();
  for(int f=threadIdx.x;f<F;f+=blockDim.x){ float s=0; for(int k=0;k<D;k++) s+=sx[k]*bf2f(w1[k*F+f]); s=rbf(s); h[t*F+f]=f2bf(s>0?s:0.f); }
}
__global__ void k_gemm2(const u16* h,const u16* w2,const float* g,const float* a,u16* out){
  int t=blockIdx.x; __shared__ float sh[F];
  for(int i=threadIdx.x;i<F;i+=blockDim.x) sh[i]=bf2f(h[t*F+i]);
  __syncthreads();
  float gg=g[t]; float og=rbf(1.f-gg);
  for(int d=threadIdx.x;d<D;d+=blockDim.x){ float s=0; for(int k=0;k<F;k++) s+=sh[k]*bf2f(w2[k*D+d]); float b=rbf(s);
    out[t*D+d]=f2bf(rbf(gg*a[t*D+d])+rbf(og*b)); }
}
extern "C" void kernel_launch(void* const* d_in, const int* in_sizes, int n_in,
                              void* d_out, int out_size, void* d_ws, size_t ws_size,
                              hipStream_t stream) {
  const u16* u=(const u16*)d_in[0];
  const u16 *Are=(const u16*)d_in[1],*Aim=(const u16*)d_in[2],*Bre=(const u16*)d_in[3],*Bim=(const u16*)d_in[4],*Cre=(const u16*)d_in[5],*Cim=(const u16*)d_in[6];
  const u16 *logdt=(const u16*)d_in[7],*Dskip=(const u16*)d_in[8];
  const u16 *w1=(const u16*)d_in[10],*w2=(const u16*)d_in[11],*gw=(const u16*)d_in[12];
  char* ws=(char*)d_ws;
  float* g=(float*)ws; float* K=(float*)(ws+65536); float* a=(float*)(ws+65536+4*D*L); u16* h=(u16*)(ws+65536+8*D*L);
  k_gate<<<L,64,0,stream>>>(u,gw,g);
  k_kgen<<<D,256,0,stream>>>(Are,Aim,Bre,Bim,Cre,Cim,logdt,K);
  k_conv<<<D,256,0,stream>>>(u,K,Dskip,a);
  k_gemm1<<<L,256,0,stream>>>(u,w1,h);
  k_gemm2<<<L,256,0,stream>>>(h,w2,g,a,(u16*)d_out);
}
```

```cpp
#include <hip/hip_runtime.h>
#include <hip/hip_bf16.h>
#include <stdint.h>

#define PRIO_S 2
#define SPIN_CAP (1u << 16)
namespace gm {
typedef unsigned short u16;
typedef unsigned int u32;
typedef unsigned long long u64;
typedef short bf16x8 __attribute__((ext_vector_type(8)));
typedef _Float16 f16x8 __attribute__((ext_vector_type(8)));
typedef short s16x4 __attribute__((ext_vector_type(4)));
typedef float f32x4 __attribute__((ext_vector_type(4)));
typedef float f32x2 __attribute__((ext_vector_type(2)));
typedef u32 u32x4 __attribute__((ext_vector_type(4)));
typedef u32 u32x2 __attribute__((ext_vector_type(2)));
typedef __amdgpu_buffer_rsrc_t rsrc_t;
#define LAS __attribute__((address_space(3)))
#define CAS __attribute__((address_space(4)))
#define DEVI __device__ __forceinline__

constexpr int L = 2048, DM = 512, NS = 128, FF = 1792;

constexpr u32 WS_W = 0;
constexpr u32 WS_TMO = 1024;
constexpr u32 WS_G = 4096;
constexpr u32 WS_F1 = 0x10000;
constexpr u32 WS_F2 = WS_F1 + 1024 * 128;
constexpr u32 WS_F3 = WS_F2 + 1024 * 128;
constexpr u32 WS_F4 = WS_F3 + 8192;
constexpr u32 WS_UT = 0x100000;
constexpr u32 WS_AT = 0x300000;
constexpr u32 WS_H = 0x500000;
constexpr u32 WS_USED = 0xC00000;
constexpr u32 WS_ST = 0x1000000;

constexpr u32 G1_STAGE = 32768, G1_W = 16384;
constexpr u32 G2_STAGE = 16384;
constexpr u32 XR = 114688;
constexpr u32 R1 = 131072;
constexpr u32 R2 = R1 + 17408;
constexpr u32 R3 = R2;
constexpr u32 KC = R2 + 12288;
constexpr u32 KCB = 1312;
constexpr u32 R4 = KC + 2 * KCB;
constexpr u32 LDS_BYTES = R4 + 64 + 256;
static_assert(LDS_BYTES <= 163840, "LDS");
constexpr u32 KROW = 272;
constexpr u32 KX = 4160;
constexpr u32 ULB = 4576;
constexpr u32 TP = 144;

struct GatherTab { u16 v[2 * 256 * 8]; };
constexpr int compact_index(int l) {
  if (l <= 256) return l;
  if (l < 512) return 128 + ((l + ((l >> 1) & 1)) >> 1);
  if (l < 1024) return 256 + ((l + 1 + ((l >> 2) & 1)) >> 2);
  return 384 + ((l + 3 + ((l >> 3) & 1)) >> 3);
}
constexpr GatherTab make_gather_tab() {
  GatherTab t{};
  for (int par = 0; par < 2; ++par)
    for (int m = 0; m < 256; ++m)
      for (int jj = 0; jj < 8; ++jj) t.v[(par * 256 + m) * 8 + jj] = (u16)(2 * (compact_index(2046 + par - 8 * m - jj) + 1));
  return t;
}
__device__ const GatherTab gather_tab = make_gather_tab();

struct Params {
  const u16 *u, *Are, *Aim, *Bre, *Bim, *Cre, *Cim, *logdt, *dskip, *w1, *w2, *gw;
  u16* out;
  char* ws;
};

template <int N> DEVI void wait_vm() { asm volatile("s_waitcnt vmcnt(%0)" ::"n"(N) : "memory"); }
DEVI void wait_lgkm0() { asm volatile("s_waitcnt lgkmcnt(0)" ::: "memory"); }
DEVI void bar() { asm volatile("" ::: "memory"); __builtin_amdgcn_s_barrier(); asm volatile("" ::: "memory"); }
DEVI void glds16(rsrc_t rs, u32 voff, u32 soff, u32 ldsaddr) {
  asm volatile("s_mov_b32 m0, %0\n\ts_nop 0\n\tbuffer_load_dwordx4 %1, %2, %3 offen lds" ::"s"(ldsaddr), "v"(voff), "s"(rs), "s"(soff) : "memory");
}
DEVI void set_m0(u32 v) { asm volatile("s_mov_b32 m0, %0\n\ts_nop 0" ::"s"(v) : "memory"); }
template <int OFF> DEVI void glds_off(rsrc_t rs, u32 voff, u32 soff) {
  asm volatile("buffer_load_dwordx4 %0, %1, %2 offen offset:%3 lds" ::"v"(voff), "s"(rs), "s"(soff), "n"(OFF) : "memory");
}
DEVI void touch(rsrc_t rs, u32 voff, u32 soff, u32 ldsdummy) {
  asm volatile("s_mov_b32 m0, %0\n\ts_nop 0\n\tbuffer_load_dword %1, %2, %3 offen lds" ::"s"(ldsdummy), "v"(voff), "s"(rs), "s"(soff) : "memory");
}
template <int OFF> DEVI u32x4 ld128co(rsrc_t rs, u32 voff, u32 soff) {
  u32x4 r; asm volatile("buffer_load_dwordx4 %0, %1, %2, %3 offen offset:%4 sc1" : "=v"(r) : "v"(voff), "s"(rs), "s"(soff), "n"(OFF) : "memory"); return r;
}
DEVI void lgkm0() { asm volatile("" ::: "memory"); __builtin_amdgcn_s_waitcnt(0xC07F); asm volatile("" ::: "memory"); }
DEVI u32x4 ld128(rsrc_t rs, u32 voff, u32 soff) { u32x4 r; asm volatile("buffer_load_dwordx4 %0, %1, %2, %3 offen" : "=v"(r) : "v"(voff), "s"(rs), "s"(soff) : "memory"); return r; }
DEVI u32x4 ld128c(rsrc_t rs, u32 voff, u32 soff) { u32x4 r; asm volatile("buffer_load_dwordx4 %0, %1, %2, %3 offen sc1" : "=v"(r) : "v"(voff), "s"(rs), "s"(soff) : "memory"); return r; }
DEVI u32x2 ld64c(rsrc_t rs, u32 voff, u32 soff) { u32x2 r; asm volatile("buffer_load_dwordx2 %0, %1, %2, %3 offen sc1" : "=v"(r) : "v"(voff), "s"(rs), "s"(soff) : "memory"); return r; }
DEVI u32 ld32c(rsrc_t rs, u32 voff, u32 soff) { u32 r; asm volatile("buffer_load_dword %0, %1, %2, %3 offen sc1" : "=v"(r) : "v"(voff), "s"(rs), "s"(soff) : "memory"); return r; }
DEVI u32 ld16(rsrc_t rs, u32 voff, u32 soff) { u32 r; asm volatile("buffer_load_ushort %0, %1, %2, %3 offen" : "=v"(r) : "v"(voff), "s"(rs), "s"(soff) : "memory"); return r; }
DEVI void st128c(rsrc_t rs, u32 voff, u32 soff, u32x4 v) { asm volatile("buffer_store_dwordx4 %0, %1, %2, %3 offen sc1\n\ts_nop 1" ::"v"(v), "v"(voff), "s"(rs), "s"(soff) : "memory"); }
#define HPOL "sc1"
DEVI void st128h(rsrc_t rs, u32 voff, u32 soff, u32x4 v) { asm volatile("buffer_store_dwordx4 %0, %1, %2, %3 offen " HPOL "\n\ts_nop 1" ::"v"(v), "v"(voff), "s"(rs), "s"(soff) : "memory"); }
DEVI void st64c(rsrc_t rs, u32 voff, u32 soff, u32x2 v) { asm volatile("buffer_store_dwordx2 %0, %1, %2, %3 offen sc1" ::"v"(v), "v"(voff), "s"(rs), "s"(soff) : "memory"); }
DEVI void st32c(rsrc_t rs, u32 voff, u32 soff, u32 v) { asm volatile("buffer_store_dword %0, %1, %2, %3 offen sc1" ::"v"(v), "v"(voff), "s"(rs), "s"(soff) : "memory"); }
#define OPOL "sc1"
DEVI void st128(rsrc_t rs, u32 voff, u32 soff, u32x4 v) { asm volatile("buffer_store_dwordx4 %0, %1, %2, %3 offen " OPOL "\n\ts_nop 1" ::"v"(v), "v"(voff), "s"(rs), "s"(soff) : "memory"); }
DEVI void st64(rsrc_t rs, u32 voff, u32 soff, u32x2 v) { asm volatile("buffer_store_dwordx2 %0, %1, %2, %3 offen" ::"v"(v), "v"(voff), "s"(rs), "s"(soff) : "memory"); }
#define PIN(x) asm volatile("" : "+v"(x))
#define SB() __builtin_amdgcn_sched_barrier(0)
DEVI f32x4 sel4(bool c, f32x4 a, f32x4 b) { asm("" : "+v"(a)); asm("" : "+v"(b)); return c ? a : b; }
template <int N, class A> DEVI void wait_vm1(A& a) { asm volatile("s_waitcnt vmcnt(%1)" : "+v"(a) : "n"(N) : "memory"); }
template <int N, class A, class B> DEVI void wait_vm2(A& a, B& b) { asm volatile("s_waitcnt vmcnt(%2)" : "+v"(a), "+v"(b) : "n"(N) : "memory"); }

DEVI float bf2f(u32 v) { return __uint_as_float(v << 16); }
DEVI float bfhi(u32 v) { return __uint_as_float(v & 0xffff0000u); }
typedef __bf16 bf16x2_t __attribute__((ext_vector_type(2)));
typedef _Float16 f16x2_t __attribute__((ext_vector_type(2)));
DEVI u32 pkbf(float lo, float hi) { return __builtin_bit_cast(u32, (bf16x2_t){(__bf16)lo, (__bf16)hi}); }
DEVI u32 pkh(float lo, float hi) { return __builtin_bit_cast(u32, (f16x2_t){(_Float16)lo, (_Float16)hi}); }
DEVI u32 pkhn(float lo, float hi) { return pkh(lo, -hi); }
DEVI f32x2 rbf2(f32x2 v) { u32 p = pkbf(v.x, v.y); asm("" : "+v"(p)); return (f32x2){bf2f(p), bfhi(p)}; }
DEVI f32x2 unpk(u32 p) { return (f32x2){bf2f(p), bfhi(p)}; }
DEVI float rbf(float f) { return bf2f(pkbf(f, f) & 0xffffu); }
#define CM1(d, a, b) "v_mul_f32 " d ", " a ", " b "\n\t"
#define CM2(d, a, b) "v_fma_f32 " d ", " a ", " b ", " d "\n\t"
#define CM2N(d, a, b) "v_fma_f32 " d ", -" a ", " b ", " d "\n\t"
DEVI void cmul2(f32x2& r0, f32x2& r1, f32x2 a0, f32x2 b0, f32x2 a1, f32x2 b1) {
  float x0, y0, x1, y1;
  asm volatile(CM1("%0", "%4", "%6") CM1("%1", "%4", "%7") CM1("%2", "%8", "%10") CM1("%3", "%8", "%11")
               CM2N("%0", "%5", "%7") CM2("%1", "%5", "%6") CM2N("%2", "%9", "%11") CM2("%3", "%9", "%10")
               : "=&v"(x0), "=&v"(y0), "=&v"(x1), "=&v"(y1)
               : "v"(a0.x), "v"(a0.y), "v"(b0.x), "v"(b0.y), "v"(a1.x), "v"(a1.y), "v"(b1.x), "v"(b1.y));
  r0 = (f32x2){x0, y0}; r1 = (f32x2){x1, y1};
}
DEVI f32x2 cmul(f32x2 a, f32x2 b) {
  float x, y;
  asm volatile(CM1("%0", "%2", "%4") CM1("%1", "%2", "%5") CM2N("%0", "%3", "%5") CM2("%1", "%3", "%4")
               : "=&v"(x), "=&v"(y) : "v"(a.x), "v"(a.y), "v"(b.x), "v"(b.y));
  return (f32x2){x, y};
}
DEVI void cmul3(f32x2& r0, f32x2& r1, f32x2& r2, f32x2 a0, f32x2 b0, f32x2 a1, f32x2 b1, f32x2 a2, f32x2 b2) {
  cmul2(r0, r1, a0, b0, a1, b1); r2 = cmul(a2, b2);
}
DEVI void cmul4(f32x2& r0, f32x2& r1, f32x2& r2, f32x2& r3, f32x2 a0, f32x2 b0, f32x2 a1, f32x2 b1, f32x2 a2, f32x2 b2, f32x2 a3, f32x2 b3) {
  cmul2(r0, r1, a0, b0, a1, b1); cmul2(r2, r3, a2, b2, a3, b3);
}
typedef short s16x2 __attribute__((ext_vector_type(2)));
typedef unsigned short u16x2 __attribute__((ext_vector_type(2)));
DEVI float dot2bf(u32 a, u32 b, float c) { return __builtin_amdgcn_fdot2_f32_bf16(__builtin_bit_cast(bf16x2_t, a), __builtin_bit_cast(bf16x2_t, b), c, false); }
template <int CTRL, int RM> DEVI float dppf(float v) { return __int_as_float(__builtin_amdgcn_update_dpp(0, __float_as_int(v), CTRL, RM, 0xf, true)); }
DEVI float wave_sum63(float v) {
  v += dppf<0x111, 0xf>(v); v += dppf<0x112, 0xf>(v); v += dppf<0x114, 0xf>(v); v += dppf<0x118, 0xf>(v);
  v += dppf<0x142, 0xa>(v); v += dppf<0x143, 0xc>(v); return v;
}
DEVI float wave_max63(float v) {
  v = fmaxf(v, dppf<0x111, 0xf>(v)); v = fmaxf(v, dppf<0x112, 0xf>(v)); v = fmaxf(v, dppf<0x114, 0xf>(v)); v = fmaxf(v, dppf<0x118, 0xf>(v));
  v = fmaxf(v, dppf<0x142, 0xa>(v)); v = fmaxf(v, dppf<0x143, 0xc>(v)); return v;
}
DEVI s16x4 tr_read(LAS char* p) { return __builtin_amdgcn_ds_read_tr16_b64_v4i16((LAS s16x4*)p); }
DEVI bf16x8 cat8(s16x4 a, s16x4 b) { return __builtin_shufflevector(a, b, 0, 1, 2, 3, 4, 5, 6, 7); }
DEVI f32x4 mfma_bf(bf16x8 a, bf16x8 b, f32x4 c) { return __builtin_amdgcn_mfma_f32_16x16x32_bf16(a, b, c, 0, 0, 0); }
DEVI f32x4 mfma_h(f16x8 a, f16x8 b, f32x4 c) { return __builtin_amdgcn_mfma_f32_16x16x32_f16(a, b, c, 0, 0, 0); }
DEVI f32x2 cexpm(float lr2, float lir, float m) {
  float a = __builtin_amdgcn_exp2f(lr2 * m);
  float ph = __builtin_amdgcn_fractf(lir * m);
  f32x2 r; r.x = a * __builtin_amdgcn_cosf(ph); r.y = a * __builtin_amdgcn_sinf(ph); return r;
}

DEVI void poll_flags(u32x2 pv, u32x2 ep, rsrc_t rsW, u32 voff, u32 soff) {
  u32 spins = 0;
  while (!__all(pv.x == ep.x && pv.y == ep.y)) {
    __builtin_amdgcn_s_sleep(1);
    if (++spins > SPIN_CAP) { st32c(rsW, 0u, WS_TMO, 1u); break; }
    pv = ld64c(rsW, voff, soff);
    wait_vm1<0>(pv);
  }
}

DEVI void hs_post(LAS char* word, u32 v, int lane) { wait_lgkm0(); if (lane == 0) *(volatile LAS u32*)word = v; }
DEVI void hs_wait(LAS char* word, u32 v) {
  for (u32 sp = 0; sp < (1u << 20); ++sp) {
    const u32 f = *(volatile LAS u32*)word;
    if ((u32)__builtin_amdgcn_readfirstlane(f) >= v) break;
  }
  asm volatile("" ::: "memory");
}
DEVI void hs_wait_all(LAS char* words, u32 v) {
  for (u32 sp = 0; sp < (1u << 20); ++sp) {
    const u32x4 f = *(volatile LAS u32x4*)words;
    const u32 a = f.x < f.y ? f.x : f.y, c = f.z < f.w ? f.z : f.w;
    if ((u32)__builtin_amdgcn_readfirstlane(a < c ? a : c) >= v) break;
  }
  asm volatile("" ::: "memory");
}
DEVI void hs_wait4(LAS char* words) {
  for (u32 sp = 0; sp < (1u << 20); ++sp) {
    const u32x4 f = *(volatile LAS u32x4*)words;
    if (__builtin_amdgcn_readfirstlane(f.x & f.y & f.z & f.w)) break;
  }
  asm volatile("" ::: "memory");
}

#define STAMP(i) do { } while (0)

constexpr int HD = 8;
constexpr int stepH(int k) { return k; }
constexpr int youngerH(int s) {
  int n = 0;
  for (int k = s + 1; k < HD; ++k) n += 2;
  for (int k = 0; k < 28 - HD; ++k) if (HD + k > s && stepH(k) < s) n += 2;
  return n;
}
constexpr int tG(int g) { return 2 * (g - 7) < g - 4 ? 2 * (g - 7) : g - 4; }
constexpr int youngerG(int st) {
  int n = 0;
  for (int g = st + 2; g < 14; ++g) if (tG(g) < st) n += 4;
  return n;
}
constexpr u32 CODE_BYTES = 35840;
template <int I> struct IC { static constexpr int v = I; };
template <int I, int N, class F> DEVI void sfor(F&& f) { if constexpr (I < N) { f(IC<I>{}); sfor<I + 1, N>(f); } }

__global__ __launch_bounds__(512, 2) void fused_kernel(Params P) {
  extern __shared__ __attribute__((aligned(16))) char lds_raw[];
  LAS char* lds = (LAS char*)lds_raw;
  const u32 ldsb = (u32)(size_t)lds;
  u64 pc0; asm volatile("s_getpc_b64 %0" : "=s"(pc0));
  asm volatile("" ::"s"(P.u), "s"(P.Are), "s"(P.Aim), "s"(P.Bre), "s"(P.Bim), "s"(P.Cre), "s"(P.Cim), "s"(P.logdt), "s"(P.dskip),
               "s"(P.w1), "s"(P.w2), "s"(P.gw), "s"(P.out), "s"(P.ws));
  const int tid = threadIdx.x, lane = tid & 63;
  const int w8 = __builtin_amdgcn_readfirstlane(tid >> 6);
  const int w = w8 & 3;
  const int b = blockIdx.x, xcd = b & 7, idx = b >> 3;
  const int l15 = lane & 15, kg = lane >> 4;
  STAMP(0);

  const rsrc_t rsW = __builtin_amdgcn_make_buffer_rsrc((void*)P.ws, 0, WS_USED, 0x00020000);
  const rsrc_t rsW2 = __builtin_amdgcn_make_buffer_rsrc((void*)P.w2, 0, DM * FF * 2, 0x00020000);
  const rsrc_t rsX = __builtin_amdgcn_make_buffer_rsrc((void*)P.u, 0, L * DM * 2, 0x00020000);
  const int rb = 4 * xcd + (idx & 3), db = idx >> 2;
  const int e0 = (idx + xcd) % 7;
  const int kh = w >> 1, th = w & 1;
  u32 v2[4];
#pragma unroll
  for (int i = 0; i < 4; ++i)
    v2[i] = (u32)(lane >> 3) * 1024 + (u32)(16 * (((lane & 7) >> 1) ^ ((lane >> 4) & 1) ^ (2 * (lane >> 5))) + 8 * (lane & 1)) * 2 + (u32)i * 7168;
  u32 g2l = ldsb + (u32)w * 4096, g2b = (u32)(128 * 7 * kh + 32 * th) * 1024 + (u32)db * 128;
  int g2e = e0;
  auto g2_piece = [&](auto ST_, auto I_) {
    constexpr int st = decltype(ST_)::v, i = decltype(I_)::v;
    int c = (st >> 1) + g2e; c = c >= 7 ? c - 7 : c;
    const u32 sp = g2b + (u32)(128 * c + 64 * (st & 1)) * 1024;
    if constexpr (i == 0) set_m0(g2l + (u32)(st % 7) * G2_STAGE);
    glds_off<i * 1024>(rsW2, v2[i], sp);
  };
  auto g2_issue = [&](auto ST_) { sfor<0, 4>([&](auto I_) { g2_piece(ST_, I_); }); };

  if (__builtin_expect(w8 >= 4, 0)) {
    if (idx < 5 && w == 0) {
      touch(__builtin_amdgcn_make_buffer_rsrc((void*)(pc0 & ~255ull), 0, CODE_BYTES, 0x00020000), (u32)lane * 128, (u32)idx * 8192, ldsb + R4 + 64);
    }
    const int cp = 32 * xcd + idx;
    const int chl = w >> 1, par = w & 1, ch = 2 * cp + chl;
    const int tb = 8 * (xcd >> 1) + (idx & 7), cgp = 4 * (xcd & 1) + (idx >> 3);
    u32x2 ep = ld64c(rsW, 0u, WS_W);
    LAS char* HS = lds + R4;
    if (w == 0 && lane < 8) *(LAS u32*)(HS + lane * 4) = 0u;
    wait_lgkm0();
    bar();
    STAMP(8);
    __builtin_amdgcn_s_setprio(PRIO_S);
    u32x4 ut0, ut1;
    {
      const u32 vo = (u32)(lane >> 3) * 1024 + (u32)(lane & 7) * 16;
      const u32 so = (u32)(64 * tb + 16 * w) * 1024 + (u32)cgp * 128;
      ut0 = ld128(rsX, vo, so);
      ut1 = ld128(rsX, vo, so + 8192);
    }
    u32 pAr, pAi, pBr, pBi, pCr, pCi;
    {
      const u32 vo = (u32)lane * 2, so = (u32)(ch * NS + 64 * par) * 2;
      pAr = ld16(__builtin_amdgcn_make_buffer_rsrc((void*)P.Are, 0, DM * NS * 2, 0x00020000), vo, so);
      pAi = ld16(__builtin_amdgcn_make_buffer_rsrc((void*)P.Aim, 0, DM * NS * 2, 0x00020000), vo, so);
      pBr = ld16(__builtin_amdgcn_make_buffer_rsrc((void*)P.Bre, 0, DM * NS * 2, 0x00020000), vo, so);
      pBi = ld16(__builtin_amdgcn_make_buffer_rsrc((void*)P.Bim, 0, DM * NS * 2, 0x00020000), vo, so);
      pCr = ld16(__builtin_amdgcn_make_buffer_rsrc((void*)P.Cre, 0, DM * NS * 2, 0x00020000), vo, so);
      pCi = ld16(__builtin_amdgcn_make_buffer_rsrc((void*)P.Cim, 0, DM * NS * 2, 0x00020000), vo, so);
    }
    if (idx >= 28 && w == 1) touch(rsW, (u32)lane * 196608, 0u, ldsb + R4 + 64);
    if (idx >= 28 && w == 2) touch(__builtin_amdgcn_make_buffer_rsrc((void*)P.out, 0, L * DM * 2, 0x00020000), (u32)lane * 262144, 0u, ldsb + R4 + 64);
    const u32 ldt2 = ((const CAS u32*)P.logdt)[cp], dsk2 = ((const CAS u32*)P.dskip)[cp];
    const float dt = rbf(expf(chl ? bfhi(ldt2) : bf2f(ldt2 & 0xffffu)));
    const float dsk = chl ? bfhi(dsk2) : bf2f(dsk2 & 0xffffu);
    asm volatile("s_waitcnt vmcnt(6)" : "+v"(ut0), "+v"(ut1)::"memory");
    {
      LAS char* T = lds + R3;
      *(LAS u32x4*)(T + (16 * w + (lane >> 3)) * TP + (lane & 7) * 16) = ut0;
      *(LAS u32x4*)(T + (16 * w + 8 + (lane >> 3)) * TP + (lane & 7) * 16) = ut1;
    }
    wait_lgkm0();
    bar();
    {
      LAS char* T = lds + R3;
      const int q = l15 >> 2, pp = l15 & 3;
#pragma unroll
      for (int hf = 0; hf < 2; ++hf) {
        LAS char* a = T + (32 * hf + 8 * kg + q) * TP + (16 * w + 4 * pp) * 2;
        st128c(rsW, (u32)l15 * 4096 + (u32)(32 * hf + 8 * kg) * 2, WS_UT + (u32)(64 * cgp + 16 * w) * 4096 + (u32)tb * 128,
               __builtin_bit_cast(u32x4, cat8(tr_read(a), tr_read(a + 4 * TP))));
      }
    }
    asm volatile("s_waitcnt vmcnt(2)" : "+v"(ep), "+v"(pAr), "+v"(pAi), "+v"(pBr), "+v"(pBi), "+v"(pCr), "+v"(pCi)::"memory");
    const u32x2 en = {ep.x + 1, ep.y + (ep.x == 0xffffffffu)};
    const float lre = dt * bf2f(pAr), lim = dt * bf2f(pAi);
    const float lr2 = lre * 1.44269504088896f, lir = lim * 0.159154943091895f;
    f32x2 c0 = cmul((f32x2){bf2f(pCr), bf2f(pCi)}, (f32x2){dt * bf2f(pBr), dt * bf2f(pBi)});
    float isc;
    {
      const float mx = wave_max63(fmaxf(fabsf(c0.x), fabsf(c0.y)));
      int eb = (int)((u32)__builtin_amdgcn_readlane(__float_as_int(mx), 63) >> 23);
      eb = eb < 12 ? 12 : (eb > 254 ? 254 : eb);
      c0 = c0 * __int_as_float((265 - eb) << 23);
      isc = __int_as_float((eb - 11) << 23);
    }
    f32x2 pw[16], ee[8], wv[17], q1, q2, q4, r[8];
    u32 cB[16], cA0[16], cA1[16], cA2[9];
    PIN(c0);
    STAMP(9);
    bar();
    pw[1] = cexpm(lr2, lir, 8.f); ee[1] = cexpm(lr2, lir, 1.f); q1 = cexpm(lr2, lir, 128.f);
    wv[0] = c0;
    cmul4(pw[2], ee[2], q2, wv[1], pw[1], pw[1], ee[1], ee[1], q1, q1, c0, q1);
    cmul4(pw[3], pw[4], ee[3], ee[4], pw[2], pw[1], pw[2], pw[2], ee[2], ee[1], ee[2], ee[2]);
    cmul3(q4, wv[2], wv[3], q2, q2, c0, q2, wv[1], q2);
    STAMP(10);
    bar();
    wait_vm<0>();
    if (lane == 0) st64c(rsW, 0u, WS_F1 + (u32)((cgp * 4 + w) * 32 + tb) * 128, en);
    u32x4 gt[4];
    {
      const rsrc_t rsT = __builtin_amdgcn_make_buffer_rsrc((void*)&gather_tab, 0, (int)sizeof(GatherTab), 0x00020000);
#pragma unroll
      for (int it = 0; it < 4; ++it) gt[it] = ld128(rsT, (u32)lane * 16, (u32)(par * 4 + it) * 1024);
    }
    cmul4(pw[5], pw[6], pw[7], pw[8], pw[4], pw[1], pw[4], pw[2], pw[4], pw[3], pw[4], pw[4]);
    cmul4(pw[9], pw[10], pw[11], pw[12], pw[8], pw[1], pw[8], pw[2], pw[8], pw[3], pw[8], pw[4]);
    cmul3(pw[13], pw[14], pw[15], pw[8], pw[5], pw[8], pw[6], pw[8], pw[7]);
    cB[0] = pkh(1.f, 0.f);
#pragma unroll
    for (int i = 1; i < 16; ++i) { cB[i] = pkh(pw[i].x, pw[i].y); PIN(cB[i]); }
    cmul3(ee[5], ee[6], ee[7], ee[4], ee[1], ee[4], ee[2], ee[4], ee[3]);
    STAMP(11);
    bar();
    cA0[0] = pkhn(wv[0].x, wv[0].y); cA0[8] = pkhn(wv[1].x, wv[1].y);
    cmul4(r[1], r[2], r[3], r[4], wv[0], ee[1], wv[0], ee[2], wv[0], ee[3], wv[0], ee[4]);
    cmul3(r[5], r[6], r[7], wv[0], ee[5], wv[0], ee[6], wv[0], ee[7]);
#pragma unroll
    for (int i = 1; i < 8; ++i) { cA0[i] = pkhn(r[i].x, r[i].y); PIN(cA0[i]); }
    cmul4(r[1], r[2], r[3], r[4], wv[1], ee[1], wv[1], ee[2], wv[1], ee[3], wv[1], ee[4]);
    cmul3(r[5], r[6], r[7], wv[1], ee[5], wv[1], ee[6], wv[1], ee[7]);
#pragma unroll
    for (int i = 1; i < 8; ++i) { cA0[8 + i] = pkhn(r[i].x, r[i].y); PIN(cA0[8 + i]); }
    PIN(cA0[0]); PIN(cA0[8]);
    STAMP(12);
    bar();
    const u32 vF1 = (u32)(lane & 31) * 128, sF1 = WS_F1 + (u32)((xcd * 4 + ((2 * idx) >> 4)) * 32) * 128;
    u32x2 pf1 = ld64c(rsW, vF1, sF1);
    cmul4(wv[4], wv[5], wv[6], wv[7], wv[0], q4, wv[1], q4, wv[2], q4, wv[3], q4);
    cmul3(r[1], r[2], r[3], wv[2], ee[2], wv[2], ee[4], wv[2], ee[6]);
    cmul3(r[5], r[6], r[7], wv[3], ee[2], wv[3], ee[4], wv[3], ee[6]);
    cA1[0] = pkhn(wv[2].x, wv[2].y); cA1[4] = pkhn(wv[3].x, wv[3].y);
#pragma unroll
    for (int i = 1; i < 4; ++i) { cA1[i] = pkhn(r[i].x, r[i].y); cA1[4 + i] = pkhn(r[4 + i].x, r[4 + i].y); }
    cmul4(r[0], r[1], r[2], r[3], wv[4], ee[4], wv[5], ee[4], wv[6], ee[4], wv[7], ee[4]);
#pragma unroll
    for (int a = 0; a < 4; ++a) { cA1[8 + 2 * a] = pkhn(wv[4 + a].x, wv[4 + a].y); cA1[9 + 2 * a] = pkhn(r[a].x, r[a].y); }
#pragma unroll
    for (int i = 0; i < 16; ++i) PIN(cA1[i]);
    bar();
    cmul4(wv[8], wv[9], wv[10], wv[11], wv[4], q4, wv[5], q4, wv[6], q4, wv[7], q4);
    cmul4(wv[12], wv[13], wv[14], wv[15], wv[8], q4, wv[9], q4, wv[10], q4, wv[11], q4);
    wv[16] = cmul(wv[12], q4);
#pragma unroll
    for (int i = 0; i <= 8; ++i) { cA2[i] = pkhn(wv[8 + i].x, wv[8 + i].y); PIN(cA2[i]); }
    wait_vm1<0>(pf1);
    poll_flags(pf1, en, rsW, vF1, sF1);
    u32x4 uv0 = ld128c(rsW, (u32)lane * 16, WS_UT + (u32)ch * 4096 + (u32)par * 2048);
    u32x4 uv1 = ld128c(rsW, (u32)lane * 16, WS_UT + (u32)ch * 4096 + (u32)par * 2048 + 1024);
    if (b == 0 && w == 3) {
      u32x2 pfa[4];
#pragma unroll
      for (int i = 0; i < 4; ++i) pfa[i] = ld64c(rsW, (u32)(lane & 31) * 128 + (u32)(lane >> 5) * (128 * 128), WS_F1 + (u32)i * (2 * 128 * 128));
      asm volatile("s_waitcnt vmcnt(0)" : "+v"(pfa[0]), "+v"(pfa[1]), "+v"(pfa[2]), "+v"(pfa[3])::"memory");
#pragma unroll
      for (int i = 0; i < 4; ++i) poll_flags(pfa[i], en, rsW, (u32)(lane & 31) * 128 + (u32)(lane >> 5) * (128 * 128), WS_F1 + (u32)i * (2 * 128 * 128));
      if (lane == 0) st64c(rsW, 0u, WS_W, en);
    }
    STAMP(13);
    bar();
    __builtin_amdgcn_s_sleep(8);
    LAS char* S = lds + R1 + (u32)w * (16 * KROW);
    LAS u32* Sw = (LAS u32*)(S + lane * 4);
    LAS char* Sr = S + l15 * KROW + kg * 16;
    constexpr int RW = KROW / 4;
    f16x8 Bf[4], Af[3][4];
#pragma unroll
    for (int i = 0; i < 16; ++i) Sw[i * RW] = cB[i];
#pragma unroll
    for (int s = 0; s < 4; ++s) Bf[s] = *(LAS f16x8*)(Sr + 64 * s);
#pragma unroll
    for (int i = 0; i < 16; ++i) Sw[i * RW] = cA0[i];
#pragma unroll
    for (int s = 0; s < 4; ++s) Af[0][s] = *(LAS f16x8*)(Sr + 64 * s);
#pragma unroll
    for (int i = 0; i < 16; ++i) Sw[i * RW] = cA1[i];
#pragma unroll
    for (int s = 0; s < 4; ++s) Af[1][s] = *(LAS f16x8*)(Sr + 64 * s);
#pragma unroll
    for (int i = 0; i <= 8; ++i) Sw[i * RW] = cA2[i];
#pragma unroll
    for (int s = 0; s < 4; ++s) Af[2][s] = *(LAS f16x8*)(Sr + 64 * s);
    f32x4 kacc[3];
#pragma unroll
    for (int t = 0; t < 3; ++t) {
      f32x4 acc = {0.f, 0.f, 0.f, 0.f};
#pragma unroll
      for (int s = 0; s < 4; ++s) acc = mfma_h(Af[t][s], Bf[s], acc);
      kacc[t] = acc * isc;
    }
    LAS f32x4* XK = (LAS f32x4*)(lds + R2);
#pragma unroll
    for (int i = 0; i < 3; ++i) XK[(w * 3 + i) * 64 + lane] = kacc[i];
    hs_post(HS + w * 4, 1u, lane);
    hs_wait_all(HS, 1u);
    STAMP(14);
    {
#pragma unroll
      for (int i = 0; i < 3; ++i) kacc[i] += XK[((w ^ 1) * 3 + i) * 64 + lane];
      LAS char* Kc = lds + KC + (u32)chl * KCB + 16;
      *(LAS u32x2*)(Kc + (128 * (kg >> 1) + 8 * l15 + 4 * (kg & 1)) * 2) = (u32x2){pkbf(kacc[0][0], kacc[0][1]), pkbf(kacc[0][2], kacc[0][3])};
      const int i0 = kg < 2 ? 256 + 64 * kg + 4 * l15 : 384 + 64 * (kg - 2) + 2 * l15;
      *(LAS u32*)(Kc + i0 * 2) = pkbf(kacc[1][0], kacc[1][1]);
      *(LAS u32*)(Kc + (i0 + (kg < 2 ? 2 : 32)) * 2) = pkbf(kacc[1][2], kacc[1][3]);
      const u32 k01 = pkbf(lane == 33 ? 0.f : kacc[2][0], kacc[2][1]), k23 = pkbf(kacc[2][2], kacc[2][3]);
      const int i2 = kg < 2 ? 512 + 64 * kg + l15 : (lane == 32 ? 640 : (lane == 33 ? -1 : 643));
      *(LAS u16*)(Kc + i2 * 2) = (u16)k01;
      *(LAS u16*)(Kc + (kg < 2 ? i2 + 16 : 643) * 2) = (u16)(k01 >> 16);
      *(LAS u16*)(Kc + (kg < 2 ? i2 + 32 : 643) * 2) = (u16)k23;
      *(LAS u16*)(Kc + (kg < 2 ? i2 + 48 : 643) * 2) = (u16)(k23 >> 16);
      asm volatile("" : "+v"(gt[0]), "+v"(gt[1]), "+v"(gt[2]), "+v"(gt[3]));
      LAS char* Kb = Kc - 2;
      LAS char* Rp = lds + R1 + (u32)(2 * chl + par) * KX;
      u16x2 g[4][4];
#pragma unroll
      for (int it = 0; it < 4; ++it)
#pragma unroll
        for (int k = 0; k < 4; ++k) {
          g[it][k][0] = *(LAS u16*)(Kb + (gt[it][k] & 0xffffu));
          g[it][k][1] = *(LAS u16*)(Kb + (gt[it][k] >> 16));
        }
#pragma unroll
      for (int it = 0; it < 4; ++it)
        *(LAS u32x4*)(Rp + (64 * it + lane) * 16) = (u32x4){__builtin_bit_cast(u32, g[it][0]), __builtin_bit_cast(u32, g[it][1]),
                                                            __builtin_bit_cast(u32, g[it][2]), __builtin_bit_cast(u32, g[it][3])};
      if (lane < 2) *(LAS u32x4*)(Rp + 4096 + lane * 16) = (u32x4){0u, 0u, 0u, 0u};
    }
    LAS char* U0 = lds + R3 + (u32)chl * 6144;
    hs_post(HS + w * 4, 2u, lane);
    hs_wait(HS + (w ^ 1) * 4, 2u);
    asm volatile("s_waitcnt vmcnt(0)" : "+v"(uv0), "+v"(uv1)::"memory");
    *(LAS u32x4*)(U0 + 480 + par * 2048 + lane * 16) = uv0;
    *(LAS u32x4*)(U0 + 480 + par * 2048 + 1024 + lane * 16) = uv1;
    if (par == 0 && lane < 30) *(LAS u32x4*)(U0 + lane * 16) = (u32x4){0u, 0u, 0u, 0u};
    hs_post(HS + w * 4, 3u, lane);
    hs_wait(HS + (w ^ 1) * 4, 3u);
    __builtin_amdgcn_s_setprio(0);
    STAMP(16);
    f32x4 ya[8];
    {
      constexpr int PD = 4;
      LAS char* U = U0 + 32 * l15 + 16 * kg + 64 * (1 - par);
      bf16x8 ub[32];
      u32 av[PD + 1][4];
      const int pr = l15 & 1;
      u32 Ag[4];
#pragma unroll
      for (int g = 0; g < 4; ++g) {
        Ag[g] = ldsb + R1 + (u32)(2 * chl + pr) * KX + 4060 - 64 * par + 16 * kg - 2 * (l15 - pr) - 128 * 31 + 1024 * g;
        PIN(Ag[g]);
      }
      auto ld = [&](auto T_, auto K_) {
        constexpr int t = decltype(T_)::v, k = decltype(K_)::v;
        if constexpr (t < 32) {
          if constexpr (k < 2) {
            LAS u32* a = (LAS u32*)(size_t)Ag[t >> 3] + 32 * (t & 7) + 2 * k;
            av[t % (PD + 1)][2 * k] = a[0]; av[t % (PD + 1)][2 * k + 1] = a[1];
          } else ub[t] = *(LAS bf16x8*)(U + 128 * t);
        }
      };
#pragma unroll
      for (int j = 0; j < 8; ++j) ya[j] = (f32x4){0.f, 0.f, 0.f, 0.f};
      sfor<0, PD>([&](auto T_) { ld(T_, IC<0>{}); ld(T_, IC<1>{}); ld(T_, IC<2>{}); });
      sfor<0, 32>([&](auto T_) {
        constexpr int t = decltype(T_)::v, ip = 31 - t, j0 = ip >> 2;
        const bf16x8 af = __builtin_bit_cast(bf16x8, (u32x4){av[t % (PD + 1)][0], av[t % (PD + 1)][1], av[t % (PD + 1)][2], av[t % (PD + 1)][3]});
        sfor<j0, 8>([&](auto J_) {
          constexpr int j = decltype(J_)::v, m = j - j0;
          ya[j] = mfma_bf(af, ub[4 * j - ip + 3], ya[j]);
          SB();
          if constexpr (m < 3) ld(IC<t + PD>{}, IC<m>{});
          SB();
        });
        sfor<(8 - j0 < 3 ? 8 - j0 : 3), 3>([&](auto K_) { ld(IC<t + PD>{}, K_); });
        SB();
      });
    }
    STAMP(17);
    {
      LAS f32x4* X = (LAS f32x4*)(lds + XR + chl * 8192);
      hs_wait4(lds + R4 + 16);
#pragma unroll
      for (int jj = 0; jj < 4; ++jj) X[(par * 4 + jj) * 64 + lane] = sel4(par, ya[2 * jj], ya[2 * jj + 1]);
      hs_post(HS + w * 4, 4u, lane);
      hs_wait(HS + (w ^ 1) * 4, 4u);
      LAS char* U = U0 + 480 + 32 * l15 + 8 * kg;
      f32x4 xo[4];
      u32x2 uu[4];
#pragma unroll
      for (int jj = 0; jj < 4; ++jj) { xo[jj] = X[((par ^ 1) * 4 + jj) * 64 + lane]; uu[jj] = *(LAS u32x2*)(U + 512 * (2 * jj + par)); }
      const f32x2 d2 = {dsk, dsk};
      u32x2 r2[4];
#pragma unroll
      for (int jj = 0; jj < 4; ++jj) {
        const f32x4 y = sel4(par, ya[2 * jj + 1], ya[2 * jj]) + xo[jj];
        const f32x2 a0 = rbf2((f32x2){y[0], y[1]}) + rbf2(unpk(uu[jj].x) * d2), a1 = rbf2((f32x2){y[2], y[3]}) + rbf2(unpk(uu[jj].y) * d2);
        r2[jj] = (u32x2){pkbf(a0.x, a0.y), pkbf(a1.x, a1.y)};
      }
#pragma unroll
      for (int jj = 0; jj < 4; ++jj) st64c(rsW, (u32)l15 * 32 + (u32)kg * 8, WS_AT + (u32)ch * 4096 + (u32)(2 * jj + par) * 512, r2[jj]);
    }
    wait_vm<0>();
    if (lane == 0) st64c(rsW, 0u, WS_F3 + (u32)(cp * 4 + w) * 8, en);
    wait_lgkm0();
    bar();
    STAMP(18);
    u32x2 pf3;
    u32 vF3a = WS_F3 + (u32)((32 * db + 8 * w) * 4 + lane) * 8, vF3b = WS_F4 + (u32)(4 * rb + w) * 8;
    PIN(vF3a); PIN(vF3b);
    const u32 vF3 = lane < 32 ? vF3a : vF3b;
    u32x4 a0, a1;
    u32 gq;
    sfor<0, 13>([&](auto T_) {
      constexpr int st = decltype(T_)::v;
      if constexpr (st + 1 >= 7 && st < 11) wait_vm<youngerG(st)>();
      if constexpr (st == 7) {
        wait_vm1<4>(pf3);
        poll_flags(pf3, en, rsW, vF3, 0u);
        const u32 va = (u32)(lane >> 3) * 4096 + (u32)(lane & 7) * 16;
        const u32 sa = WS_AT + (u32)(64 * db + 16 * w) * 4096 + (u32)rb * 128;
        a0 = ld128c(rsW, va, sa); a1 = ld128c(rsW, va, sa + 8 * 4096);
        gq = ld32c(rsW, (u32)l15 * 4, WS_G + (u32)(64 * rb + 16 * w) * 4);
      }
      if constexpr (st == 11) wait_vm<0>();
      bar();
      if constexpr (st == 5) pf3 = ld64c(rsW, vF3, 0u);
      sfor<7, 14>([&](auto G_) { if constexpr (tG(decltype(G_)::v) == st) g2_issue(G_); });
    });
    {
      asm volatile("s_waitcnt vmcnt(0)" : "+v"(a0), "+v"(a1), "+v"(gq)::"memory");
      LAS char* T = lds + R3;
      *(LAS u32x4*)(T + (16 * w + (lane >> 3)) * TP + (lane & 7) * 16) = a0;
      *(LAS u32x4*)(T + (16 * w + 8 + (lane >> 3)) * TP + (lane & 7) * 16) = a1;
      if (lane < 16) *(LAS u32*)(lds + R4 + 64 + (16 * w + lane) * 4) = gq;
    }
    wait_lgkm0();
    bar();
    STAMP(19);
    } else {
    const bool g1on = idx < 28;
    LAS char* GH = lds + R4 + 16;
    u32x2 ep, en;
    if (__builtin_expect(!g1on, 0)) {
      const int gw = ((xcd >> 1) * 8 + (xcd & 1) * 4 + (idx - 28)) * 4 + w;
      u32x4 v[8];
#define WAIT_V(N) asm volatile("s_waitcnt vmcnt(" #N ")" : "+v"(v[0]), "+v"(v[1]), "+v"(v[2]), "+v"(v[3]), "+v"(v[4]), "+v"(v[5]), "+v"(v[6]), "+v"(v[7])::"memory")
      u32x4 gwv = ld128(__builtin_amdgcn_make_buffer_rsrc((void*)P.gw, 0, DM * 2, 0x00020000), (u32)lane * 16, 0u);
      ep = ld64c(rsW, 0u, WS_W);
      float zs = 0.f;
#pragma unroll
      for (int hf = 0; hf < 2; ++hf) {
#pragma unroll
        for (int i = 0; i < 8; ++i) v[i] = ld128(rsX, (u32)lane * 16, (u32)(16 * gw + 8 * hf + i) * 1024);
        bar(); bar();
        asm volatile("s_waitcnt vmcnt(8)" : "+v"(gwv), "+v"(ep)::"memory");
        WAIT_V(0);
        float sm[8];
#pragma unroll
        for (int i = 0; i < 8; ++i) {
          float s = 0.f;
#pragma unroll
          for (int j = 0; j < 4; ++j) s = dot2bf(pkbf(bf2f(v[i][j] & 0xffffu) * bf2f(gwv[j] & 0xffffu), bfhi(v[i][j]) * bfhi(gwv[j])), 0x3f803f80u, s);
          sm[i] = s;
        }
#pragma unroll
        for (int i = 0; i < 8; ++i) sm[i] = wave_sum63(sm[i]);
#pragma unroll
        for (int i = 0; i < 8; ++i) {
          const float z = __int_as_float(__builtin_amdgcn_readlane(__float_as_int(sm[i]), 63));
          zs = lane == 8 * hf + i ? z : zs;
        }
      }
#undef WAIT_V
      en = (u32x2){ep.x + 1, ep.y + (ep.x == 0xffffffffu)};
      const float gv = rbf(__builtin_amdgcn_rcpf(1.f + __builtin_amdgcn_exp2f(rbf(zs) * -1.44269504088896f)));
      if (lane < 16) st32c(rsW, (u32)lane * 4, WS_G + (u32)(16 * gw) * 4, __float_as_uint(gv));
      bar(); bar(); bar(); bar();
      wait_vm<0>();
      if (lane == 0) { st64c(rsW, 0u, WS_F4 + (u32)gw * 8, en); *(volatile LAS u32*)(GH + w * 4) = 1u; }
      sfor<0, 3>(g2_issue);
    } else {
      const int p1 = 4 * (xcd >> 1) + (idx & 3), c1 = 7 * (xcd & 1) + (idx >> 2);
      const int s0 = 2 * (idx & 3);
      const rsrc_t rsW1 = __builtin_amdgcn_make_buffer_rsrc((void*)P.w1, 0, DM * FF * 2, 0x00020000);
      u32 vx[4], vw[4];
#pragma unroll
      for (int i = 0; i < 4; ++i) {
        vx[i] = (u32)(lane >> 3) * 1024 + (u32)(((lane & 7) ^ (lane >> 3)) * 16) + (u32)i * 7168;
        const int q = lane >> 4, pc = lane & 15, n = (pc >> 1) ^ q ^ (4 * (i & 1));
        vw[i] = (u32)q * (FF * 2) + (u32)(16 * n + 8 * (pc & 1)) * 2 + (u32)i * (8 * FF * 2 - 1024);
      }
      const u32 bx = (u32)(128 * p1 + 32 * w) * 1024;
      const u32 bw = (u32)(32 * (w >> 1) + 4 * (w & 1)) * (FF * 2) + (u32)c1 * 256;
      auto g1_piece = [&](auto ST_, auto I_) {
        constexpr int st = decltype(ST_)::v, i = decltype(I_)::v;
        const u32 ck = (u32)((st + s0) & 7);
        if constexpr (i == 0) set_m0(ldsb + (u32)(st & 3) * G1_STAGE + (u32)w * 4096);
        if constexpr (i == 4) set_m0(ldsb + (u32)(st & 3) * G1_STAGE + G1_W + (u32)w * 4096);
        if constexpr (i < 4) glds_off<i * 1024>(rsX, vx[i], bx + ck * 128);
        else glds_off<(i - 4) * 1024>(rsW1, vw[i - 4], bw + ck * (64 * FF * 2));
      };
      sfor<0, 8>([&](auto I_) { g1_piece(IC<0>{}, I_); });
      sfor<0, 8>([&](auto I_) { g1_piece(IC<1>{}, I_); });
      ep = ld64c(rsW, 0u, WS_W);
      f32x4 acc1[4][4];
#pragma unroll
      for (int tt = 0; tt < 4; ++tt)
#pragma unroll
        for (int n = 0; n < 4; ++n) acc1[tt][n] = (f32x4){0.f, 0.f, 0.f, 0.f};
      LAS char* xa0 = lds + (64 * kh + l15) * 128 + ((0 + kg) ^ (l15 & 7)) * 16;
      LAS char* xa1 = lds + (64 * kh + l15) * 128 + ((4 + kg) ^ (l15 & 7)) * 16;
      u32 wa[4];
#pragma unroll
      for (int n = 0; n < 4; ++n)
        wa[n] = G1_W + kg * 1024 + (l15 >> 2) * 256 + (((4 * th + n) ^ (l15 >> 2) ^ (4 * (kg & 1))) * 32) + (l15 & 3) * 8;
      bf16x8 fa[2][4], fb[2][4];
      auto rdA = [&](int buf, int n, int slot, int ks) {
        LAS char* a = lds + wa[n] + slot * G1_STAGE + ks * 8192;
        fa[buf][n] = cat8(tr_read(a), tr_read(a + 4096));
      };
      auto rdB = [&](int buf, int tt, int slot, int ks) { fb[buf][tt] = *(LAS bf16x8*)((ks ? xa1 : xa0) + slot * G1_STAGE + tt * 2048); };
      auto kstep = [&](auto CUR_, auto RD_, int slot, int ks, auto&& ld) {
        constexpr int cur = decltype(CUR_)::v, nx = cur ^ 1;
        constexpr bool rd = decltype(RD_)::v;
        sfor<0, 8>([&](auto N_) {
          constexpr int g = decltype(N_)::v, n = g >> 1, t0 = 2 * (g & 1);
          acc1[t0][n] = mfma_bf(fa[cur][n], fb[cur][t0], acc1[t0][n]);
          SB();
          if constexpr (rd && g < 4) { rdB(nx, g, slot, ks); }
          SB();
          acc1[t0 + 1][n] = mfma_bf(fa[cur][n], fb[cur][t0 + 1], acc1[t0 + 1][n]);
          SB();
          if constexpr (rd && g < 4) { rdA(nx, g, slot, ks); }
          ld(N_);
          SB();
        });
      };
      wait_vm<9>();
      bar();
      STAMP(1);
#pragma unroll
      for (int n = 0; n < 4; ++n) { rdA(0, n, 0, 0); rdB(0, n, 0, 0); }
      sfor<0, 8>([&](auto S_) {
        constexpr int s = decltype(S_)::v;
        lgkm0();
        kstep(IC<0>{}, IC<1>{}, s & 3, 1, [&](auto G_) {
          constexpr int g = decltype(G_)::v;
          if constexpr (s == 0) g1_piece(IC<2>{}, G_);
          if constexpr (s >= 1 && s <= 4 && (g & 1)) g1_piece(IC<s + 3>{}, IC<(g >> 1)>{});
          if constexpr (s == 5 && (g & 1)) g2_piece(IC<0>{}, IC<(g >> 1)>{});
        });
        lgkm0();
        if constexpr (s == 0) { wait_vm1<8>(ep); en = (u32x2){ep.x + 1, ep.y + (ep.x == 0xffffffffu)}; }
        else if constexpr (s <= 3) wait_vm<12>();
        else if constexpr (s == 4) wait_vm<12>();
        else if constexpr (s == 5) wait_vm<12>();
        else if constexpr (s == 6) wait_vm<8>();
        if constexpr (s < 7) bar();
        else if (lane == 0) *(volatile LAS u32*)(GH + w * 4) = 1u;
        kstep(IC<1>{}, IC<(s < 7)>{}, (s + 1) & 3, 0, [&](auto G_) {
          constexpr int g = decltype(G_)::v;
          if constexpr (s == 0) g1_piece(IC<3>{}, G_);
          if constexpr (s >= 1 && s <= 4 && (g & 1)) g1_piece(IC<s + 3>{}, IC<4 + (g >> 1)>{});
          if constexpr (s == 5 && (g & 1)) g2_piece(IC<1>{}, IC<(g >> 1)>{});
        });
      });
      STAMP(2);
#pragma unroll
      for (int tt = 0; tt < 4; ++tt) {
        const u32 base = WS_H + (u32)(((8 * p1 + 4 * kh + tt) * 14 + c1) * 4 + 2 * th) * 1024;
#pragma unroll
        for (int pr = 0; pr < 2; ++pr) {
          const f32x4 a = acc1[tt][2 * pr], c = acc1[tt][2 * pr + 1];
          st128h(rsW, (u32)lane * 16, base + (u32)pr * 1024,
                 (u32x4){pkbf(fmaxf(a[0], 0.f), fmaxf(a[1], 0.f)), pkbf(fmaxf(a[2], 0.f), fmaxf(a[3], 0.f)),
                         pkbf(fmaxf(c[0], 0.f), fmaxf(c[1], 0.f)), pkbf(fmaxf(c[2], 0.f), fmaxf(c[3], 0.f))});
        }
      }
      hs_wait4(GH);
      g2_issue(IC<2>{});
      wait_vm<4>();
      if (lane == 0) st64c(rsW, 0u, WS_F2 + (u32)(((p1 * 2 + kh) * 14 + c1) * 2 + th) * 128, en);
    }
    STAMP(3);
    const int l14 = min(lane, 13);
    const u32 vF2 = (u32)l14 * 128, sF2 = WS_F2 + (u32)((rb * 14 + 7 * kh) * 2) * 128;
    asm volatile("" : "+s"(g2l), "+s"(g2b), "+s"(g2e));
    u32x2 pf2 = ld64c(rsW, vF2, sF2);
    sfor<3, 7>(g2_issue);
    wait_vm1<16>(pf2);
    poll_flags(pf2, en, rsW, vF2, sF2);
    STAMP(4);
    f32x4 acc2[2][4];
#pragma unroll
    for (int tt = 0; tt < 2; ++tt)
#pragma unroll
      for (int n = 0; n < 4; ++n) acc2[tt][n] = (f32x4){0.f, 0.f, 0.f, 0.f};
    u32x4 hq[HD][2];
    const u32 hb0 = WS_H + (u32)((4 * rb + 2 * th) * 14 + 7 * kh) * 4096;
    const u32 vh0 = (u32)lane * 16, vh1 = vh0 + 14 * 4096;
    auto h_issue = [&](auto S_, auto TT_) {
      constexpr int s = decltype(S_)::v, r = s % HD, tt = decltype(TT_)::v;
      int c = (s >> 2) + e0; c = c >= 7 ? c - 7 : c;
      hq[r][tt] = ld128co<(s & 3) * 1024>(rsW, tt ? vh1 : vh0, hb0 + (u32)c * 4096);
    };
    sfor<0, HD>([&](auto S_) { h_issue(S_, IC<0>{}); h_issue(S_, IC<1>{}); });
    u32 wb[4];
#pragma unroll
    for (int n = 0; n < 4; ++n)
      wb[n] = kh * 8192 + kg * 512 + (l15 >> 2) * 128 + ((n ^ (l15 >> 3) ^ (2 * (kg & 1))) * 32) + (l15 & 3) * 8;
    bf16x8 fa[2][4];
    auto rd1 = [&](int buf, int s, int n) {
      LAS char* a = lds + wb[n] + ((s >> 1) % 7) * G2_STAGE + (s & 1) * 4096;
      fa[buf][n] = cat8(tr_read(a), tr_read(a + 2048));
    };
    wait_vm<2 * HD>();
    bar();
    STAMP(5);
#pragma unroll
    for (int n = 0; n < 4; ++n) rd1(0, 0, n);
    sfor<0, 28>([&](auto S_) {
      constexpr int s = decltype(S_)::v, st = s >> 1, sub = s & 1, r = s % HD;
      lgkm0();
      if constexpr (sub == 1 && st < 13) bar();
      asm volatile("s_waitcnt vmcnt(%2)" : "+v"(hq[r][0]), "+v"(hq[r][1]) : "n"(youngerH(s)) : "memory");
      const bf16x8 hb[2] = {__builtin_bit_cast(bf16x8, hq[r][0]), __builtin_bit_cast(bf16x8, hq[r][1])};
      sfor<0, 4>([&](auto N_) {
        constexpr int n = decltype(N_)::v;
        acc2[0][n] = mfma_bf(fa[sub][n], hb[0], acc2[0][n]);
        SB();
        if constexpr (s + 1 < 28 && n < 2) rd1(sub ^ 1, s + 1, 2 * n);
        SB();
        acc2[1][n] = mfma_bf(fa[sub][n], hb[1], acc2[1][n]);
        SB();
        if constexpr (s + 1 < 28 && n < 2) rd1(sub ^ 1, s + 1, 2 * n + 1);
        SB();
      });
      sfor<0, 28 - HD>([&](auto K_) { constexpr int k = decltype(K_)::v; if constexpr (stepH(k) == s) { h_issue(IC<HD + k>{}, IC<0>{}); h_issue(IC<HD + k>{}, IC<1>{}); } });
    });
    STAMP(6);
    LAS f32x4* X = (LAS f32x4*)(lds + XR);
#pragma unroll
    for (int n = 0; n < 4; ++n) X[(w * 4 + n) * 64 + lane] = sel4(kh, acc2[0][n], acc2[1][n]);
    wait_lgkm0();
    bar();
    STAMP(7);
    const int tl = 2 * th + kh;
    LAS char* T = lds + R3;
    const int q = l15 >> 2, pp = l15 & 3;
    const float g = *(LAS float*)(lds + R4 + 64 + (16 * tl + l15) * 4);
    f32x4 xo[4];
    u32x2 au[4];
#pragma unroll
    for (int n = 0; n < 4; ++n) {
      xo[n] = X[((w ^ 2) * 4 + n) * 64 + lane];
      au[n] = __builtin_bit_cast(u32x2, tr_read(T + (16 * n + 4 * kg + q) * TP + (16 * tl + 4 * pp) * 2));
    }
    const float og = rbf(1.f - g);
    const f32x2 g2 = {g, g}, og2 = {og, og};
    u32 ro[4][2];
#pragma unroll
    for (int n = 0; n < 4; ++n) {
      const f32x4 bsum = sel4(kh, acc2[1][n], acc2[0][n]) + xo[n];
      const f32x2 o0 = rbf2(g2 * unpk(au[n].x)) + rbf2(og2 * rbf2((f32x2){bsum[0], bsum[1]}));
      const f32x2 o1 = rbf2(g2 * unpk(au[n].y)) + rbf2(og2 * rbf2((f32x2){bsum[2], bsum[3]}));
      ro[n][0] = pkbf(o0.x, o0.y); ro[n][1] = pkbf(o1.x, o1.y);
    }
    const rsrc_t rsO = __builtin_amdgcn_make_buffer_rsrc((void*)P.out, 0, L * DM * 2, 0x00020000);
#pragma unroll
    for (int a = 0; a < 2; ++a) {
      const auto s0_ = __builtin_amdgcn_permlane16_swap(ro[2 * a][0], ro[2 * a + 1][0], false, false);
      const auto s1_ = __builtin_amdgcn_permlane16_swap(ro[2 * a][1], ro[2 * a + 1][1], false, false);
      st128(rsO, (u32)l15 * 1024 + (u32)((kg & 1) * 32 + (kg >> 1) * 16), (u32)(64 * rb + 16 * tl) * 1024 + (u32)(64 * db + 32 * a) * 2,
            (u32x4){s0_[0], s1_[0], s0_[1], s1_[1]});
    }
    STAMP(19);
  }
}
}

extern "C" void kernel_launch(void* const* d_in, const int* in_sizes, int n_in, void* d_out, int out_size, void* d_ws,
                              size_t ws_size, hipStream_t stream) {
  using namespace gm;
  Params p;
  p.u = (const u16*)d_in[0];
  p.Are = (const u16*)d_in[1]; p.Aim = (const u16*)d_in[2];
  p.Bre = (const u16*)d_in[3]; p.Bim = (const u16*)d_in[4];
  p.Cre = (const u16*)d_in[5]; p.Cim = (const u16*)d_in[6];
  p.logdt = (const u16*)d_in[7]; p.dskip = (const u16*)d_in[8];
  p.w1 = (const u16*)d_in[10]; p.w2 = (const u16*)d_in[11]; p.gw = (const u16*)d_in[12];
  p.out = (u16*)d_out; p.ws = (char*)d_ws;
  (void)hipFuncSetAttribute((const void*)fused_kernel, hipFuncAttributeMaxDynamicSharedMemorySize, LDS_BYTES);
  fused_kernel<<<256, 512, LDS_BYTES, stream>>>(p);
}
```
